```python
import torch

def _softmax(x, axis=-1):
    return torch.softmax(x, dim=axis)
def _gelu(x):
    return torch.nn.functional.gelu(x, approximate="tanh")
def _silu(x):
    return torch.nn.functional.silu(x)
def _sigmoid(x):
    return torch.sigmoid(x)
def _layernorm(x, axis=-1, eps=1e-5):
    m = x.mean(dim=axis, keepdim=True)
    v = x.var(dim=axis, keepdim=True, unbiased=False)
    return (x - m) / torch.sqrt(v + eps)
def _scatter_add(tgt, idx, src, axis=0):
    return tgt.clone().index_add_(axis, idx, src)
def _segment_sum(src, seg, n):
    out = torch.zeros((n,) + src.shape[1:], dtype=src.dtype)
    return out.index_add_(0, seg, src)
def _pad(x, pw):
    flat = tuple(v for pair in reversed(pw) for v in pair)
    return torch.nn.functional.pad(x, flat)

kernel_name = 'interleave_layers__ssm_conv_s4__sliding_block__prompt'

def setup_inputs():

    _w = (lambda a: (lambda t: t.to(torch.bfloat16) if t.is_floating_point() else t)(torch.as_tensor(a)))
    def _A_setup():
        B, L, D, N = 1, 2048, 512, 128
        A_re = -(lambda _x: torch.exp(torch.as_tensor(_x)))((lambda *s: torch.randn(*s, dtype=torch.bfloat16) * 1.6)(D, N))
        A_im = torch.pi * (lambda *s: torch.randn(*s, dtype=torch.bfloat16) * 0.05)(D, N)
        B_re = (lambda *s: torch.randn(*s, dtype=torch.bfloat16) * 0.05)(D, N)
        B_im = (lambda *s: torch.randn(*s, dtype=torch.bfloat16) * 0.05)(D, N)
        C_re = (lambda *s: torch.randn(*s, dtype=torch.bfloat16) * 0.05)(D, N)
        C_im = (lambda *s: torch.randn(*s, dtype=torch.bfloat16) * 0.05)(D, N)
        log_dt = (lambda *s: torch.randn(*s, dtype=torch.bfloat16) * 0.05)(D) * 0.5 - 4.0
        Dskip = (lambda *s: torch.randn(*s, dtype=torch.bfloat16) * 0.05)(D)
        return {
            "u": _w((lambda *s: torch.randn(*s, dtype=torch.bfloat16) * 0.05)(B, L, D)),
            "A_re": _w(A_re),
            "A_im": _w(A_im),
            "B_re": _w(B_re),
            "B_im": _w(B_im),
            "C_re": _w(C_re),
            "C_im": _w(C_im),
            "log_dt": _w(log_dt),
            "Dskip": _w(Dskip),
        }
    _A_in = _A_setup()

    def _B_setup():
        B, S, D = 1, 2048, 512
        return {
            "x": _w((lambda *s: torch.randn(*s, dtype=torch.bfloat16) * 0.05)(B, S, D)),
            "wqkv": _w((lambda *s: torch.randn(*s, dtype=torch.bfloat16) * 0.05)(D, 3 * D)),
            "wo": _w((lambda *s: torch.randn(*s, dtype=torch.bfloat16) * 0.05)(D, D)),
        }
    _B_in = _B_setup()

    return {
        "A_u": _A_in["u"],
        "A_A_re": _A_in["A_re"],
        "A_A_im": _A_in["A_im"],
        "A_B_re": _A_in["B_re"],
        "A_B_im": _A_in["B_im"],
        "A_C_re": _A_in["C_re"],
        "A_C_im": _A_in["C_im"],
        "A_log_dt": _A_in["log_dt"],
        "A_Dskip": _A_in["Dskip"],
        "B_x": _B_in["x"],
        "B_wqkv": _B_in["wqkv"],
        "B_wo": _B_in["wo"],
    }

def reference(A_u, A_A_re, A_A_im, A_B_re, A_B_im, A_C_re, A_C_im, A_log_dt, A_Dskip, B_x, B_wqkv, B_wo):
    def _A_ref(u, A_re, A_im, B_re, B_im, C_re, C_im, log_dt, Dskip):
        Bsz, L, D = u.shape
        dt = torch.exp(log_dt)[:, None]
        A = A_re + 1j * A_im
        Bc = B_re + 1j * B_im
        Cc = C_re + 1j * C_im
        logdA = dt * A
        dB = dt * Bc
        l = (lambda x, dt: x.to(dt))(torch.arange(L), A_re.dtype)
        P = torch.exp(logdA[:, :, None] * l[None, None, :])
        K = torch.einsum("dn,dnl,dn->dl", Cc, P, dB).real
        L2 = 2 * L
        Kp = _pad(K, ((0, 0), (0, L)))
        up = (lambda x, *d: x.permute(*d))(u, 0, 2, 1)
        up = _pad(up, ((0, 0), (0, 0), (0, L)))
        Kf = (lambda x, axes=(-2,-1): torch.fft.rfft2(x.float(), dim=axes))(Kp[:, None, :], axes=(-2, -1))
        Uf = (lambda x, axes=(-2,-1): torch.fft.rfft2(x.float(), dim=axes))(up[:, :, None, :], axes=(-2, -1))
        Yf = Uf * Kf[None, :, :, :]
        y = (lambda x, s, axes=(-2,-1): torch.fft.irfft2(x, s=s, dim=axes).to(torch.bfloat16))(Yf, (1, L2), axes=(-2, -1))[:, :, 0, :L]
        y = (lambda x, *d: x.permute(*d))(y, 0, 2, 1) + u * Dskip[None, None, :]
        return y

    def _B_ref(x, wqkv, wo):
        B, S, D = x.shape
        H = 32; Dh = D // H; W = 256
        qkv = (lambda x, *d: x.permute(*d))((x @ wqkv).reshape(B, S, 3, H, Dh), 2, 0, 3, 1, 4)
        q, k, v = qkv[0], qkv[1], qkv[2]
        scores = torch.einsum("bhqd,bhkd->bhqk", q, k) * (Dh ** -0.5)
        idx = torch.arange(S)
        mask = (idx[:, None] - idx[None, :] >= 0) & (idx[:, None] - idx[None, :] < W)
        scores = torch.where(mask[None, None], scores, -1e9)
        p = _softmax(scores, axis=-1)
        o = torch.einsum("bhqk,bhkd->bhqd", p, v)
        return (lambda x, *d: x.permute(*d))(o, 0, 2, 1, 3).reshape(B, S, D) @ wo

    x = A_u
    x = x + _A_ref(_layernorm(x, axis=-1), A_A_re, A_A_im, A_B_re, A_B_im, A_C_re, A_C_im, A_log_dt, A_Dskip)
    x = x + _B_ref(_layernorm(x, axis=-1), B_wqkv, B_wo)
    x = x + _A_ref(_layernorm(x, axis=-1), A_A_re, A_A_im, A_B_re, A_B_im, A_C_re, A_C_im, A_log_dt, A_Dskip)
    x = x + _B_ref(_layernorm(x, axis=-1), B_wqkv, B_wo)
    return x
```

```cpp
#include <hip/hip_runtime.h>
#include <hip/hip_bf16.h>
#include <stdint.h>

typedef __attribute__((ext_vector_type(8))) short    bf16x8;
typedef __attribute__((ext_vector_type(8))) _Float16 f16x8;
typedef __attribute__((ext_vector_type(16))) float   f32x16;
typedef __attribute__((ext_vector_type(4))) float    f32x4;
typedef __attribute__((ext_vector_type(2))) float    f32x2;
typedef __attribute__((ext_vector_type(4))) unsigned u32x4;
typedef __attribute__((ext_vector_type(2))) unsigned u32x2;
typedef __attribute__((ext_vector_type(2))) __bf16   bf2v;
typedef unsigned short u16;

#define DI __device__ __forceinline__
#if defined(__HIP_DEVICE_COMPILE__)
#define NOPK __attribute__((target("no-packed-fp32-ops")))
#else
#define NOPK
#endif

constexpr int L_ = 2048, D_ = 512, NS_ = 128;
#define RAW_BARRIER() do { asm volatile("s_waitcnt lgkmcnt(0)" ::: "memory"); __builtin_amdgcn_s_barrier(); asm volatile("" ::: "memory"); } while (0)

DI unsigned f2u(float v) { return __builtin_bit_cast(unsigned, v); }
DI float u2f(unsigned v) { return __builtin_bit_cast(float, v); }
DI float sum32(float v) { auto r = __builtin_amdgcn_permlane32_swap(f2u(v), f2u(v), false, false); return u2f(r[0]) + u2f(r[1]); }
DI float max32(float v) { auto r = __builtin_amdgcn_permlane32_swap(f2u(v), f2u(v), false, false); return fmaxf(u2f(r[0]), u2f(r[1])); }
DI float bf2f(unsigned v) { return u2f(v << 16); }
DI float bflo(unsigned w) { return u2f(w << 16); }
DI float bfhi(unsigned w) { return u2f(w & 0xffff0000u); }
DI unsigned pack_bf2(float a, float b) {
  f32x2 v = {a, b};
  return __builtin_bit_cast(unsigned, __builtin_convertvector(v, bf2v));
}
DI float bfr(float a) { return bflo(pack_bf2(a, 0.f)); }

#define MFMA_BF(a, b, c) __builtin_amdgcn_mfma_f32_32x32x16_bf16((a), (b), (c), 0, 0, 0)
#define MFMA_F16(a, b, c) __builtin_amdgcn_mfma_f32_32x32x16_f16((a), (b), (c), 0, 0, 0)

constexpr size_t WS_XT0 = 0;
constexpr size_t WS_XT2 = WS_XT0 + (2u << 20);
constexpr size_t WS_ST0 = WS_XT2 + (2u << 20);
constexpr size_t WS_ST2 = WS_ST0 + (64u << 10);
constexpr size_t WS_R   = WS_ST2 + (64u << 10);
constexpr int    RLEN   = 2080;
constexpr size_t WS_KS  = WS_R + (size_t)512 * RLEN * 2;
constexpr size_t WS_WF  = WS_KS + 4096;
constexpr size_t WS_CS  = WS_WF + (size_t)1536 * 512 * 2;
constexpr size_t WS_WOF = WS_CS + 8192;
constexpr size_t WS_XF  = WS_WOF + (size_t)512 * 512 * 2;
constexpr size_t WS_QF  = WS_XF + (2u << 20);
constexpr size_t WS_KF  = WS_QF + (2u << 20);
constexpr size_t WS_VF  = WS_KF + (2u << 20);
constexpr size_t WS_OF  = WS_VF + (2u << 20);
constexpr size_t WS_FLG = WS_OF + (2u << 20);
constexpr int    NFLG   = 2 * 256 + 2 * 64;
constexpr size_t WS_END = WS_FLG + (size_t)NFLG * 256;
#define STAMP(kid, i) do {} while (0)

DI float rowmax16(float v) {
  v = fmaxf(v, u2f(__builtin_amdgcn_update_dpp(0u, f2u(v), 0x111, 0xf, 0xf, true)));
  v = fmaxf(v, u2f(__builtin_amdgcn_update_dpp(0u, f2u(v), 0x112, 0xf, 0xf, true)));
  v = fmaxf(v, u2f(__builtin_amdgcn_update_dpp(0u, f2u(v), 0x114, 0xf, 0xf, true)));
  v = fmaxf(v, u2f(__builtin_amdgcn_update_dpp(0u, f2u(v), 0x118, 0xf, 0xf, true)));
  return v;
}
DI float rowsum16(float v) {
  v += u2f(__builtin_amdgcn_update_dpp(0u, f2u(v), 0x111, 0xf, 0xf, true));
  v += u2f(__builtin_amdgcn_update_dpp(0u, f2u(v), 0x112, 0xf, 0xf, true));
  v += u2f(__builtin_amdgcn_update_dpp(0u, f2u(v), 0x114, 0xf, 0xf, true));
  v += u2f(__builtin_amdgcn_update_dpp(0u, f2u(v), 0x118, 0xf, 0xf, true));
  return v;
}
#define LDS_MAX(ptr, v) asm volatile("ds_max_u32 %0, %1" :: "v"((unsigned)(size_t)(ptr)), "v"(f2u(v)) : "memory")

struct cplx { float r, i; };
DI cplx cmul(cplx a, cplx b) { return {a.r * b.r - a.i * b.i, a.r * b.i + a.i * b.r}; }
DI cplx cexp_l(float lr, float li, float l) {
  const float xr = lr * l, x = li * l;
  const float t = xr * 1.4426950216293335f;
  const float er = fmaf(xr, 1.4426950216293335f, -t) + xr * 1.9259629911e-8f;
  const float e = __builtin_amdgcn_exp2f(t) * fmaf(er, 0.6931471805599453f, 1.f);
  const float k = rintf(x * 0.63661977236758134f);
  float r = fmaf(k, -1.57079637050628662109375f, x);
  r = fmaf(k, 4.37113900018624283e-8f, r);
  const float z = r * r;
  const float S = fmaf(fmaf(fmaf(-1.9515295891e-4f, z, 8.3321608736e-3f), z, -1.6666654611e-1f) * z, r, r);
  const float C = fmaf(fmaf(fmaf(2.443315711809948e-5f, z, -1.388731625493765e-3f), z, 4.166664568298827e-2f) * z, z, fmaf(-0.5f, z, 1.f));
  const int q = (int)k;
  float sn = (q & 1) ? C : S, cs = (q & 1) ? S : C;
  sn = (q & 2) ? -sn : sn;
  cs = ((q + 1) & 2) ? -cs : cs;
  return {e * cs, e * sn};
}

DI void ln_ms(float s1, float s2, float& m_bf, float& s_bf) {
  const float mean = s1 * (1.f / 512.f);
  float var = s2 * (1.f / 512.f) - mean * mean;
  var = fmaxf(var, 0.f);
  m_bf = bfr(mean);
  s_bf = bfr(__builtin_amdgcn_sqrtf(bfr(bfr(var) + 1e-5f)));
}
DI void ln_stats(float s1, float s2, float& m_bf, float& rinv) {
  float s; ln_ms(s1, s2, m_bf, s);
  rinv = __builtin_amdgcn_rcpf(s);
}

constexpr int KG_RS = 264;
constexpr int K0_FAC = (48 + 16) * KG_RS * 2;
constexpr int K0_ANC = K0_FAC + 128 * 24 * 2 + 64 + 2 * 648 * 4;
constexpr int K0_LDS = K0_ANC + 4096;

typedef __attribute__((ext_vector_type(2))) _Float16 f16x2;
DI void kg_store(u16* m, int row, int n, float a, float b) {
  f16x2 h = {(_Float16)a, (_Float16)b};
  *(f16x2*)(m + row * KG_RS + 2 * n) = h;
}

__global__ __launch_bounds__(512) NOPK void k0_prep(
    const u16* __restrict__ x0, const u16* __restrict__ Are, const u16* __restrict__ Aim,
    const u16* __restrict__ Bre, const u16* __restrict__ Bim, const u16* __restrict__ Cre,
    const u16* __restrict__ Cim, const u16* __restrict__ logdt, const u16* __restrict__ wqkv,
    const u16* __restrict__ wo, char* __restrict__ ws) {
  extern __shared__ __attribute__((aligned(16))) char smem[];
  const int tid = threadIdx.x, b = blockIdx.x;
  STAMP(0, 0);
  const int d = b;
  const __amdgpu_buffer_rsrc_t rs0 = __builtin_amdgcn_make_buffer_rsrc(ws, 0, (int)WS_END, 0x00020000);
  const int w = __builtin_amdgcn_readfirstlane(tid >> 6), lane = tid & 63;
  const int n = tid & 127, part = w >> 1;
  const unsigned gi = (unsigned)(d * NS_ + n);
  if (b < NFLG / 2 && tid < 64) *(unsigned*)(ws + WS_FLG + (size_t)(2 * b + (tid >> 5)) * 256 + (tid & 31) * 4) = 0u;
#define K0_LD16(dst, p) asm volatile("global_load_ushort %0, %1, off" : "=v"(dst) : "v"(p))
#define K0_WAITP(n) asm volatile("s_waitcnt vmcnt(" #n ")" ::: "memory")
#define K0_CSLD() do { const unsigned o_ = (unsigned)(tid >> 5) * 32 * 1536 + b * 64 + 2 * (tid & 31); _Pragma("unroll") \
    for (int r = 0; r < 32; ++r) asm volatile("global_load_dword %0, %1, off" : "=v"(csv[r]) : "v"(wqkv + (o_ + (unsigned)r * 1536))); } while (0)
  unsigned v_ld, v_ar, v_ai, v_br, v_bi, v_cr, v_ci;
  K0_LD16(v_ld, logdt + d); K0_LD16(v_ar, Are + gi); K0_LD16(v_ai, Aim + gi); K0_LD16(v_br, Bre + gi);
  K0_LD16(v_bi, Bim + gi); K0_LD16(v_cr, Cre + gi); K0_LD16(v_ci, Cim + gi);
  unsigned lv[8];
  u32x4 xv;
  unsigned csv[32];
  u32x4 xs;
  const int t0 = (b >> 4) * 64, d0 = (((b & 7) << 1) | ((b >> 3) & 1)) * 32;
  if (w < 4) {
    const int kap = b & 31, g = b >> 5;
    const u16* src = w < 3 ? wqkv + w * 512 : wo;
    const unsigned rs = w < 3 ? 1536u : 512u;
    const unsigned o = (unsigned)(16 * kap + 4 * (lane >> 5)) * rs + g * 32 + (lane & 31);
#pragma unroll
    for (int j = 0; j < 8; ++j) K0_LD16(lv[j], src + (o + (unsigned)(8 * (j >> 2) + (j & 3)) * rs));
    if (b < 24) { K0_CSLD(); K0_WAITP(40); } else K0_WAITP(8);
  } else {
    const unsigned i = tid - 256;
    asm volatile("global_load_dwordx4 %0, %1, off" : "=v"(xv) : "v"(x0 + ((unsigned)(t0 * D_ + d0) + (i >> 2) * D_ + 8 * (i & 3))));
    asm volatile("global_load_dwordx4 %0, %1, off" : "=v"(xs) : "v"(x0 + ((unsigned)b * (4 * D_) + 8 * i)));
    if (b < 24) { K0_CSLD(); K0_WAITP(34); } else K0_WAITP(2);
  }
  asm volatile("" : "+v"(v_ld), "+v"(v_ar), "+v"(v_ai), "+v"(v_br), "+v"(v_bi), "+v"(v_cr), "+v"(v_ci));
  u16* Am = (u16*)smem;
  u16* Bm = Am + 48 * KG_RS;
  float* Kd = (float*)(smem + K0_FAC + 128 * 24 * 2 + 64);
  u16* tile = (u16*)(smem + K0_FAC);
  float* red = (float*)(smem + K0_FAC + 128 * 24 * 2);
  f32x2* anc = (f32x2*)(smem + K0_ANC);
  float gsc, csa = 0.f, csb = 0.f;
  {
    const float dt = bfr(expf(bf2f(v_ld)));
    const float lr = dt * bf2f(v_ar), li = dt * bf2f(v_ai);
    const cplx dB = {dt * bf2f(v_br), dt * bf2f(v_bi)};
    const cplx Cc = {bf2f(v_cr), bf2f(v_ci)};
    cplx CB = cmul(Cc, dB);
    const cplx Ea = cexp_l(lr, li, part == 0 ? 1.f : (part == 1 ? 8.f : (part == 2 ? 128.f : 1024.f)));
    { f32x2 v = {Ea.r, Ea.i}; anc[part * 128 + n] = v; }
    if (part == 3) {
      const float m2 = Ea.r * Ea.r + Ea.i * Ea.i;
      float bnd = (fabsf(CB.r) + fabsf(CB.i)) * fmaxf(1.f, m2);
      bnd = rowmax16(bnd);
      if ((lane & 15) == 15) red[4 + (n >> 4)] = bnd;
      if (tid == 511) { ((unsigned*)red)[0] = 0u; ((unsigned*)red)[1] = 0u; }
    }
  if (w >= 4) {
    const int i = tid - 256;
    float s1 = 0.f, s2 = 0.f;
    if (b < 24) K0_WAITP(32); else K0_WAITP(0);
    asm volatile("" : "+v"(xv), "+v"(xs));
#pragma unroll
    for (int j = 0; j < 4; ++j) {
      float a = bflo(xs[j]), c = bfhi(xs[j]);
      s1 += a + c; s2 += a * a + c * c;
    }
    u32x2 lo = {xv[0], xv[1]}, hi = {xv[2], xv[3]};
    *(u32x2*)(tile + (i >> 2) * 36 + 8 * (i & 3)) = lo; *(u32x2*)(tile + (i >> 2) * 36 + 8 * (i & 3) + 4) = hi;
    s1 = rowsum16(s1); s2 = rowsum16(s2);
#define RL(v, l) u2f(__builtin_amdgcn_readlane(f2u(v), l))
    s1 = (RL(s1, 15) + RL(s1, 31)) + (RL(s1, 47) + RL(s1, 63));
    s2 = (RL(s2, 15) + RL(s2, 31)) + (RL(s2, 47) + RL(s2, 63));
    float mb, sb; ln_ms(s1, s2, mb, sb);
    if (lane == 0) ((unsigned*)(ws + WS_ST0))[4 * b + w - 4] = (f2u(mb) >> 16) | (f2u(sb) & 0xffff0000u);
  }
    RAW_BARRIER();
  if (b < 24) {
    asm volatile("s_waitcnt vmcnt(0)" : "+v"(csv[0]), "+v"(csv[1]), "+v"(csv[2]), "+v"(csv[3]), "+v"(csv[4]), "+v"(csv[5]), "+v"(csv[6]), "+v"(csv[7]),
                 "+v"(csv[8]), "+v"(csv[9]), "+v"(csv[10]), "+v"(csv[11]), "+v"(csv[12]), "+v"(csv[13]), "+v"(csv[14]), "+v"(csv[15]));
    asm volatile("" : "+v"(csv[16]), "+v"(csv[17]), "+v"(csv[18]), "+v"(csv[19]), "+v"(csv[20]), "+v"(csv[21]), "+v"(csv[22]), "+v"(csv[23]),
                 "+v"(csv[24]), "+v"(csv[25]), "+v"(csv[26]), "+v"(csv[27]), "+v"(csv[28]), "+v"(csv[29]), "+v"(csv[30]), "+v"(csv[31]));
#pragma unroll
    for (int r = 0; r < 32; ++r) { csa += bflo(csv[r]); csb += bfhi(csv[r]); }
  }
  if (w < 4) {
    asm volatile("s_waitcnt vmcnt(0)" : "+v"(lv[0]), "+v"(lv[1]), "+v"(lv[2]), "+v"(lv[3]), "+v"(lv[4]), "+v"(lv[5]), "+v"(lv[6]), "+v"(lv[7]));
    u32x4 o = {lv[0] | (lv[1] << 16), lv[2] | (lv[3] << 16), lv[4] | (lv[5] << 16), lv[6] | (lv[7] << 16)};
    const int off = w < 3 ? (int)WS_WF + (b >> 5) * 98304 + (w == 0 ? 65536 + (b & 31) * 1024 : ((b & 31) * 2 + w - 1) * 1024) + lane * 16
                          : (int)WS_WOF + (b * 64 + lane) * 16;
    __builtin_amdgcn_raw_buffer_store_b128(o, rs0, off, 0, 16);
  } else {
    const int i = tid - 256, dd = i >> 3, c = i & 7;
    u32x4 v;
#pragma unroll
    for (int k = 0; k < 4; ++k) v[k] = tile[(8 * c + 2 * k) * 36 + dd] | ((unsigned)tile[(8 * c + 2 * k + 1) * 36 + dd] << 16);
    __builtin_amdgcn_raw_buffer_store_b128(v, rs0, (int)WS_XT0 + ((d0 + dd) * L_ + t0 + 8 * c) * 2, 0, 16);
  }
    {
      const f32x4 b0 = *(const f32x4*)(red + 4), b1 = *(const f32x4*)(red + 8);
      const float bnd = fmaxf(fmaxf(fmaxf(b0[0], b0[1]), fmaxf(b0[2], b0[3])), fmaxf(fmaxf(b1[0], b1[1]), fmaxf(b1[2], b1[3])));
      int ex = 0;
      if (bnd > 0.f && bnd < 3.0e38f) { (void)frexpf(bnd, &ex); }
      ex = 14 - ex; ex = ex > 120 ? 120 : (ex < -120 ? -120 : ex);
      gsc = ldexpf(1.f, -ex);
      const float sc = ldexpf(1.f, ex);
      CB.r *= sc; CB.i *= sc;
    }
    const f32x2 a128 = anc[256 + n];
    const cplx E128 = {a128[0], a128[1]};
    if (part == 0) {
      cplx E[8];
      E[0] = {1.f, 0.f};
      E[1] = Ea;
      E[2] = cmul(E[1], E[1]); E[3] = cmul(E[2], E[1]); E[4] = cmul(E[2], E[2]);
      E[5] = cmul(E[4], E[1]); E[6] = cmul(E[4], E[2]); E[7] = cmul(E[4], E[3]);
      const cplx P1 = cmul(CB, E128);
#pragma unroll
      for (int k = 0; k < 8; ++k) {
        cplx g = k ? cmul(CB, E[k]) : CB; kg_store(Am, k, n, g.r, -g.i);
        g = k ? cmul(P1, E[k]) : P1;      kg_store(Am, 8 + k, n, g.r, -g.i);
      }
    } else if (part == 1) {
      cplx Lw[16];
      Lw[0] = {1.f, 0.f}; Lw[1] = Ea;
#pragma unroll
      for (int k = 2; k < 16; ++k) Lw[k] = cmul(Lw[k >> 1], Lw[k - (k >> 1)]);
#pragma unroll
      for (int k = 0; k < 16; ++k) kg_store(Bm, k, n, Lw[k].r, Lw[k].i);
    } else if (part == 2) {
      const f32x2 a1 = anc[n];
      const cplx E1 = {a1[0], a1[1]};
      const cplx E2 = cmul(E1, E1), E4 = cmul(E2, E2), E6 = cmul(E4, E2);
      cplx P = cmul(CB, cmul(E128, E128));
#pragma unroll
      for (int a = 0; a < 2; ++a) {
        kg_store(Am, 16 + 4 * a, n, P.r, -P.i);
        cplx g = cmul(P, E2); kg_store(Am, 17 + 4 * a, n, g.r, -g.i);
        g = cmul(P, E4);      kg_store(Am, 18 + 4 * a, n, g.r, -g.i);
        g = cmul(P, E6);      kg_store(Am, 19 + 4 * a, n, g.r, -g.i);
        P = cmul(P, E128);
      }
#pragma unroll
      for (int a = 0; a < 4; ++a) {
        kg_store(Am, 24 + 2 * a, n, P.r, -P.i);
        cplx g = cmul(P, E4); kg_store(Am, 25 + 2 * a, n, g.r, -g.i);
        if (a < 3) P = cmul(P, E128);
      }
    } else {
      cplx P = cmul(CB, Ea);
#pragma unroll
      for (int a = 0; a < 9; ++a) {
        kg_store(Am, 32 + a, n, P.r, -P.i);
        if (a < 8) P = cmul(P, E128);
      }
    }
  }
  RAW_BARRIER();
  STAMP(0, 1);
  if (w < 6) {
    const int rtile = w % 3, kh = w / 3;
    const int ro = (16 * rtile + (lane & 15)) * KG_RS + 8 * (lane >> 4) + 128 * kh;
    const int co = (lane & 15) * KG_RS + 8 * (lane >> 4) + 128 * kh;
    const int ml = lane & 15, g = lane >> 4;
    int c0, dr, dr2;
    if (rtile == 0)      { c0 = (g >> 1) * 128 + (g & 1) * 4 + 8 * ml; dr = 1; dr2 = 2; }
    else if (rtile == 1) { c0 = g < 2 ? 256 + g * 64 + 4 * ml : 384 + (g - 2) * 64 + 2 * ml; dr = 1; dr2 = g < 2 ? 2 : 32; }
    else                 { c0 = 512 + g * 64 + ml; dr = 16; dr2 = 32; }
    asm volatile("" : "+v"(c0), "+v"(dr), "+v"(dr2));
    f16x8 af[4], bf[4];
#pragma unroll
    for (int ks = 0; ks < 4; ++ks) { af[ks] = *(const f16x8*)(Am + ro + 32 * ks); bf[ks] = *(const f16x8*)(Bm + co + 32 * ks); }
    f32x4 acc = {0.f, 0.f, 0.f, 0.f};
#pragma unroll
    for (int ks = 0; ks < 4; ++ks) acc = __builtin_amdgcn_mfma_f32_16x16x32_f16(af[ks], bf[ks], acc, 0, 0, 0);
    float mx = 0.f;
#pragma unroll
    for (int r = 0; r < 4; ++r) {
      const int ci = c0 + (r & 1) * dr + (r >> 1) * dr2;
      if (ci <= 640) { Kd[kh * 648 + ci] = acc[r]; mx = fmaxf(mx, fabsf(acc[r])); }
    }
    mx = rowmax16(mx);
    if (ml == 15) LDS_MAX(red + kh, mx);
  }
  RAW_BARRIER();
  STAMP(0, 2);
  const float mx = red[0] + red[1];
  int ex = 0;
  if (mx > 0.f && mx < 3.0e38f) { (void)frexpf(mx, &ex); }
  ex = 15 - ex;
  ex = ex > 100 ? 100 : (ex < -100 ? -100 : ex);
  const float scale = ldexpf(1.f, ex);
  _Float16* R = (_Float16*)(ws + WS_R) + (size_t)d * RLEN;
  {
    typedef __attribute__((ext_vector_type(4))) _Float16 f16x4;
    f16x4 o;
    const int l0 = 2047 - 4 * tid;
    const float lf = (float)l0;
    const unsigned ra = pack_bf2(lf, lf - 1.f), rb = pack_bf2(lf - 2.f, lf - 3.f);
    const unsigned rr[4] = {ra & 0xffffu, ra >> 16, rb & 0xffffu, rb >> 16};
#pragma unroll
    for (int j = 0; j < 4; ++j) {
      const int ci = l0 < 128 ? l0 - j : (int)rr[j] - 17024;
      o[j] = (_Float16)((Kd[ci] + Kd[648 + ci]) * scale);
    }
    *(f16x4*)(R + 4 * tid) = o;
  }
  if (tid < 4) { u32x4 z = {0u, 0u, 0u, 0u}; *(u32x4*)(R + 2048 + 8 * tid) = z; }
  if (tid == 0) ((float*)(ws + WS_KS))[d] = ldexpf(1.f, -ex - 9) * gsc;
  STAMP(0, 3);
  if (b < 24) {
    float* csr = (float*)smem;
    csr[(tid >> 5) * 64 + 2 * (tid & 31)] = csa; csr[(tid >> 5) * 64 + 2 * (tid & 31) + 1] = csb;
    RAW_BARRIER();
    if (tid < 64) {
      float sacc = 0.f;
#pragma unroll
      for (int k = 0; k < 16; ++k) sacc += csr[k * 64 + tid];
      ((float*)(ws + WS_CS))[b * 64 + tid] = sacc;
    }
  }
  STAMP(0, 4);
}

constexpr int CV_S = 268;
constexpr int CV_KC = 2 * 8 * CV_S * 16;
constexpr int CV_ST = 4 * 260 * 16;
constexpr int CV_PS = 100 * 16;
constexpr int CV_PL = 2 * 4 * CV_PS;
constexpr int CV_X = 2 * 8192;
constexpr int CV_LDS = CV_KC + CV_ST + CV_PL + CV_X;
constexpr int CV_RED = 64 * 36;

__global__ __launch_bounds__(512) NOPK void k_conv(const u16* __restrict__ xt, const f32x2* __restrict__ stp,
                                                   const u16* __restrict__ dskip, char* __restrict__ ws, int FIN) {
  extern __shared__ __attribute__((aligned(16))) char smem[];
  char* kc = smem;
  f32x4* sst = (f32x4*)(smem + CV_KC);
  char* pl = smem + CV_KC + CV_ST;
  unsigned* xs = (unsigned*)(smem + CV_KC + CV_ST + CV_PL);
  const int tid = threadIdx.x;
  STAMP(1, 0);
  const int cp = (blockIdx.x & 7) * 32 + (blockIdx.x >> 3);
  const int ch = tid >> 8, j = tid & 255;
  const int d = 2 * cp + ch;
  const u16* R = (const u16*)(ws + WS_R) + (unsigned)(d * RLEN + 8 * j);
  const u32x4 r0 = *(const u32x4*)(R);
  const u32x4 r1 = *(const u32x4*)(R + 8);
  f32x4 sp[8];
  typedef f32x4 __attribute__((aligned(8))) f32x4u;
  const unsigned ta = FIN ? 4 * j : 2 * tid, tb = FIN ? 0 : 2 * tid;
#pragma unroll
  for (int k = 0; k < 8; ++k) sp[k] = *(const f32x4u*)(stp + (FIN ? (k & 1) * 2 : (k >> 1) * 2048 + (k & 1) * 1024) + (k < 2 ? ta : tb));
  const u32x4 xv = *(const u32x4*)(xt + (unsigned)(d * L_ + 8 * j));
  const f32x2 kinv = *(const f32x2*)((const float*)(ws + WS_KS) + 2 * cp);
  const unsigned dk = *(const unsigned*)(dskip + 2 * cp);
  __builtin_amdgcn_sched_barrier(0);
  {
    unsigned wv[8] = {r0[0], r0[1], r0[2], r0[3], r1[0], r1[1], r1[2], r1[3]};
    char* base = kc + ch * 8 * CV_S * 16 + j * 16;
#pragma unroll
    for (int c = 0; c < 8; ++c) {
      u32x4 o;
#pragma unroll
      for (int k = 0; k < 4; ++k) {
        const int wi = (c >> 1) + k;
        o[k] = (c & 1) ? __builtin_amdgcn_alignbit(wv[wi + 1], wv[wi], 16) : wv[wi];
      }
      *(u32x4*)(base + c * CV_S * 16) = o;
    }
    u32x4 z = {0u, 0u, 0u, 0u};
    if (j < 32) *(u32x4*)(kc + ch * 8 * CV_S * 16 + (j >> 2) * CV_S * 16 + (256 + (j & 3)) * 16) = z;
    if (j >= 128) *(u32x4*)(pl + ch * 4 * CV_PS + ((j >> 5) & 3) * CV_PS + (j & 31) * 16) = z;
  }
  f32x4 st4[4];
  if (FIN) {
#pragma unroll
    for (int k = 0; k < 4; ++k) {
      const unsigned a0 = f2u(sp[k >> 1][2 * (k & 1)]), a1 = f2u(sp[k >> 1][2 * (k & 1) + 1]);
      f32x4 o = {bflo(a0), __builtin_amdgcn_rcpf(bfhi(a0)), bflo(a1), __builtin_amdgcn_rcpf(bfhi(a1))};
      st4[k] = o;
    }
  } else {
#pragma unroll
    for (int k = 0; k < 2; ++k) {
      const f32x4 a = (sp[k] + sp[2 + k]) + (sp[4 + k] + sp[6 + k]);
      float m0, q0, m1, q1; ln_stats(a[0], a[1], m0, q0); ln_stats(a[2], a[3], m1, q1);
      f32x4 o = {m0, q0, m1, q1}; sst[(tid & 3) * 260 + k * 128 + (tid >> 2)] = o;
    }
    RAW_BARRIER();
#pragma unroll
    for (int k = 0; k < 4; ++k) st4[k] = sst[k * 260 + j];
  }
  STAMP(1, 1);
  {
    f16x8 uo;
    u32x4 xa, xb;
#pragma unroll
    for (int k = 0; k < 4; ++k) {
      const f32x4 s = st4[k];
      const unsigned up = pack_bf2(bfr(bflo(xv[k]) - s[0]) * s[1], bfr(bfhi(xv[k]) - s[2]) * s[3]);
      uo[2 * k] = (_Float16)(bflo(up) * 512.f);
      uo[2 * k + 1] = (_Float16)(bfhi(up) * 512.f);
      const unsigned e0 = __builtin_amdgcn_perm(up, xv[k], 0x05040100u), e1 = __builtin_amdgcn_perm(up, xv[k], 0x07060302u);
      if (k < 2) { xa[2 * k] = e0; xa[2 * k + 1] = e1; } else { xb[2 * k - 4] = e0; xb[2 * k - 3] = e1; }
    }
    *(f16x8*)(pl + ch * 4 * CV_PS + (j & 3) * CV_PS + ((j >> 2) + 32) * 16) = uo;
    *(u32x4*)(xs + ch * 2048 + 8 * j) = xa;
    *(u32x4*)(xs + ch * 2048 + 8 * j + 4) = xb;
  }
  RAW_BARRIER();
  STAMP(1, 2);
  const int w = tid >> 6, lane = tid & 63;
  const int wch = w >> 2, sub = w & 3;
  const int li = lane & 31, h = lane >> 5;
  f32x16 D0, D1;
#pragma unroll
  for (int r = 0; r < 16; ++r) { D0[r] = 0.f; D1[r] = 0.f; }
  {
    const char* ab = kc + wch * 8 * CV_S * 16 + (7 - (li & 7)) * CV_S * 16 + (255 - (li >> 3) + h) * 16 - sub * 64;
    const char* bb = pl + wch * 4 * CV_PS + h * CV_PS + (li + 32 - sub) * 16;
    f16x8 F[2][4];
#define CV_LOAD(buf, st) do { const int s_ = 4 * ((st) >> 1), ks_ = (st) & 1; \
      F[buf][0] = *(const f16x8*)(ab - (2 * s_ - ks_) * 32); F[buf][1] = *(const f16x8*)(ab - (2 * (s_ + 32) - ks_) * 32); \
      F[buf][2] = *(const f16x8*)(bb + ks_ * 2 * CV_PS + (32 - s_) * 16); F[buf][3] = *(const f16x8*)(bb + ks_ * 2 * CV_PS - s_ * 16); } while (0)
    CV_LOAD(0, 0);
#pragma unroll
    for (int st = 0; st < 16; ++st) {
      if (st < 15) CV_LOAD((st + 1) & 1, st + 1);
      __builtin_amdgcn_sched_barrier(0);
      D1 = MFMA_F16(F[st & 1][0], F[st & 1][2], D1);
      D0 = MFMA_F16(F[st & 1][0], F[st & 1][3], D0);
      D1 = MFMA_F16(F[st & 1][1], F[st & 1][3], D1);
      __builtin_amdgcn_sched_barrier(0);
    }
  }
  RAW_BARRIER();
  STAMP(1, 3);
  float* red = (float*)smem;
  {
    float* rw = red + w * CV_RED;
#pragma unroll
    for (int g = 0; g < 4; ++g) {
      f32x4 v0 = {D0[4 * g], D0[4 * g + 1], D0[4 * g + 2], D0[4 * g + 3]};
      f32x4 v1 = {D1[4 * g], D1[4 * g + 1], D1[4 * g + 2], D1[4 * g + 3]};
      *(f32x4*)(rw + li * 36 + 8 * g + 4 * h) = v0;
      *(f32x4*)(rw + (32 + li) * 36 + 8 * g + 4 * h) = v1;
    }
  }
  RAW_BARRIER();
  STAMP(1, 4);
  {
    const float dk0 = bflo(dk), dk1 = bfhi(dk);
    const int dd = (2 * cp) & 15, kap = (2 * cp) >> 4;
    const int hh = (dd >> 2) & 1, jj = (dd & 3) + 4 * (dd >> 3);
    u16* xf = (u16*)(ws + WS_XF) + (unsigned)((((tid >> 5) * 32 + kap) * 64 + (tid & 31) + 32 * hh) * 8 + jj);
    const float* rp = red + (tid >> 5) * 36 + (tid & 31);
#pragma unroll
    for (int i = 0; i < 4; ++i) {
      const float* r = rp + i * 576;
      const float c0 = (r[0] + r[CV_RED]) + (r[2 * CV_RED] + r[3 * CV_RED]);
      const float c1 = (r[4 * CV_RED] + r[5 * CV_RED]) + (r[6 * CV_RED] + r[7 * CV_RED]);
      const unsigned e0 = xs[tid + 512 * i], e1 = xs[2048 + tid + 512 * i];
      const unsigned ya = pack_bf2(c0 * kinv[0], c1 * kinv[1]);
      const unsigned yb = pack_bf2(bfhi(e0) * dk0, bfhi(e1) * dk1);
      const unsigned y = pack_bf2(bflo(ya) + bflo(yb), bfhi(ya) + bfhi(yb));
      *(unsigned*)(xf + i * (16 * 32 * 64 * 8)) = pack_bf2(bflo(e0) + bflo(y), bflo(e1) + bfhi(y));
    }
  }
  STAMP(1, 5);
}

typedef __attribute__((address_space(1))) unsigned gu32;
#define RLX_AGENT __ATOMIC_RELAXED, __HIP_MEMORY_SCOPE_AGENT
typedef __attribute__((ext_vector_type(2))) __bf16 bf2d;

constexpr int FB_KV = 0;
constexpr int FB_XCH = 98304;
constexpr int FB_MR = FB_XCH + 57344;
constexpr int FB_CONST = FB_MR + 2048;
constexpr int FB_LDS = FB_CONST + 64;

DI void wait_all(gu32* p, int n, int lane) {
  gu32* q = p + (lane < n ? lane : 0);
#pragma unroll 1
  for (unsigned spins = 0; spins < (1u << 20); ++spins) {
    if (__all(__hip_atomic_load(q, RLX_AGENT) != 0u)) return;
    __builtin_amdgcn_s_sleep(6);
  }
}

#define GLOAD16(dst, ptr, off) asm volatile("global_load_dwordx4 %0, %1, off offset:%2" : "=v"(dst) : "v"(ptr), "i"(off) : "memory")
DI void glds16(const char* gsrc, unsigned ldsaddr) {
  unsigned keep;
  asm volatile("s_mov_b32 %0, m0\n\ts_mov_b32 m0, %2\n\ts_nop 0\n\tglobal_load_lds_dwordx4 %1, off\n\ts_mov_b32 m0, %0"
               : "=&s"(keep) : "v"(gsrc), "s"(ldsaddr) : "memory");
}

#define MAX16(dst, sv) do { \
    float a_ = fmaxf(fmaxf(sv[0], sv[1]), sv[2]), c_ = fmaxf(fmaxf(sv[3], sv[4]), sv[5]); \
    a_ = fmaxf(fmaxf(a_, sv[6]), sv[7]);   c_ = fmaxf(fmaxf(c_, sv[8]), sv[9]); \
    a_ = fmaxf(fmaxf(a_, sv[10]), sv[11]); c_ = fmaxf(fmaxf(c_, sv[12]), sv[13]); \
    a_ = fmaxf(fmaxf(a_, sv[14]), sv[15]); dst = fmaxf(a_, c_); } while (0)
#define EXP8(p, sc, o_) do { _Pragma("unroll") for (int k = 0; k < 4; ++k) \
    p[k] = pack_bf2(__builtin_amdgcn_exp2f(sc[(o_) + 2 * k]), __builtin_amdgcn_exp2f(sc[(o_) + 2 * k + 1])); } while (0)
DI void attn_tiles(const bf16x8 q, const char* kp, const char* vp, int vstep, unsigned vs2, int n, int li, int h, f32x16& o) {
  const float NEG = -1.0e30f;
  bf16x8 kf[9];
#pragma unroll
  for (int j = 0; j < 9; ++j) kf[j] = *(const bf16x8*)(kp - j * 4096);
  f32x16 cz;
#pragma unroll
  for (int r = 0; r < 16; ++r) { cz[r] = 0.f; o[r] = 0.f; }
  const int lim = li - 4 * h;
  float mb;
  {
    f32x16 s0 = MFMA_BF(kf[0], q, cz), s8 = MFMA_BF(kf[8], q, cz);
#pragma unroll
    for (int r = 0; r < 16; ++r) s0[r] = ((r & 3) + 8 * (r >> 2) <= lim) ? s0[r] : (n == 9 ? s8[r] : NEG);
    MAX16(mb, s0);
  }
#pragma unroll
  for (int j = 1; j < 8; ++j) {
    const f32x16 sv = MFMA_BF(kf[j], q, cz);
    float a; MAX16(a, sv);
    a = fmaxf(a, mb);
    mb = j < n ? a : mb;
  }
  mb = max32(mb);
#pragma unroll
  for (int r = 0; r < 16; ++r) cz[r] = -mb;
  f32x16 S[2];
  bf16x8 vv[2][2];
  S[1] = MFMA_BF(kf[1], q, cz);
  vv[1][0] = *(const bf16x8*)(vp - vstep); vv[1][1] = *(const bf16x8*)(vp - vstep + vs2);
  {
    f32x16 s0 = MFMA_BF(kf[0], q, cz), s8 = MFMA_BF(kf[8], q, cz);
    const bf16x8 a0 = *(const bf16x8*)(vp), a1 = *(const bf16x8*)(vp + vs2);
    const bf16x8 e0 = *(const bf16x8*)(vp - 8 * vstep), e1 = *(const bf16x8*)(vp - 8 * vstep + vs2);
#pragma unroll
    for (int r = 0; r < 16; ++r) s0[r] = ((r & 3) + 8 * (r >> 2) <= lim) ? s0[r] : (n == 9 ? s8[r] : NEG);
    u32x4 p0, p1, d0, d1;
    EXP8(p0, s0, 0); EXP8(p1, s0, 8);
#pragma unroll
    for (int k = 0; k < 4; ++k) {
      const int ra = 2 * (k & 1) + 8 * (k >> 1);
      const unsigned m0 = (ra <= lim ? 0xffffu : 0u) | (ra + 1 <= lim ? 0xffff0000u : 0u);
      const unsigned m1 = (ra + 16 <= lim ? 0xffffu : 0u) | (ra + 17 <= lim ? 0xffff0000u : 0u);
      d0[k] = p0[k] & m0; p0[k] &= ~m0; d1[k] = p1[k] & m1; p1[k] &= ~m1;
    }
    o = MFMA_BF(a0, __builtin_bit_cast(bf16x8, d0), o);
    o = MFMA_BF(a1, __builtin_bit_cast(bf16x8, d1), o);
    if (n == 9) {
      o = MFMA_BF(e0, __builtin_bit_cast(bf16x8, p0), o);
      o = MFMA_BF(e1, __builtin_bit_cast(bf16x8, p1), o);
    }
  }
#pragma unroll
  for (int j = 1; j < 8; ++j) {
    if (j < n) {
      if (j < 7) {
        S[(j + 1) & 1] = MFMA_BF(kf[j + 1], q, cz);
        vv[(j + 1) & 1][0] = *(const bf16x8*)(vp - (j + 1) * vstep);
        vv[(j + 1) & 1][1] = *(const bf16x8*)(vp - (j + 1) * vstep + vs2);
      }
      __builtin_amdgcn_sched_barrier(0);
      const f32x16 sc = S[j & 1];
      u32x4 p0, p1;
      EXP8(p0, sc, 0);
      o = MFMA_BF(vv[j & 1][0], __builtin_bit_cast(bf16x8, p0), o);
      EXP8(p1, sc, 8);
      o = MFMA_BF(vv[j & 1][1], __builtin_bit_cast(bf16x8, p1), o);
      __builtin_amdgcn_sched_barrier(0);
    }
  }
}

__global__ __launch_bounds__(512, 2) NOPK void k_blk(char* __restrict__ ws, u16* __restrict__ out, int FINAL) {
  extern __shared__ __attribute__((aligned(16))) char smem[];
  const int tid = threadIdx.x, lane = tid & 63;
  const int w = __builtin_amdgcn_readfirstlane(tid >> 6);
  const int b = blockIdx.x;
  STAMP(2, 0);
  const int rt = ((b & 7) >> 1) * 4 + ((b >> 3) & 3), hp = (b >> 6) * 4 + (b & 1) * 2 + ((b >> 5) & 1);
  const int kh = w >> 2, ts = kh ? 7 - w : w;
  const int tt = 4 * rt + ts;
  const int li = lane & 31, h = lane >> 5;
  const __amdgpu_buffer_rsrc_t rs = __builtin_amdgcn_make_buffer_rsrc(ws, 0, (int)WS_END, 0x00020000);
  gu32* flg = (gu32*)(ws + WS_FLG) + (FINAL ? 256 * 64 : 0);
  gu32* cntb = (gu32*)(ws + WS_FLG) + (512 + (FINAL ? 64 : 0)) * 64;
  f32x16 ak, av;
  float s1 = 0.f, s2 = 0.f, s1b = 0.f, s2b = 0.f;
  u32x4 cq0, cq1, ck0, ck1; unsigned cvu;
  const int tt5 = 4 * rt + (hp & 3), dtile = 4 * (hp >> 2) + ts;
  u32x4 wo[16], xres;
  u32x4 xr[16];
  const char* wp = ws + WS_WOF + ((size_t)(dtile * 32 + kh * 16) * 64 + lane) * 16;
  {
    const unsigned lds0 = (unsigned)(size_t)smem;
    const char* wsrc = ws + WS_WF + (size_t)hp * 98304 + kh * 32768 + (w & 3) * 2048 + lane * 16;
    const unsigned wdst = lds0 + kh * 32768 + (w & 3) * 2048;
    const char* qsrc = ws + WS_WF + (size_t)hp * 98304 + 65536 + w * 4096 + lane * 16;
    const unsigned qdst = lds0 + 65536 + w * 4096;
    const char* xsrc = ws + WS_XF + ((size_t)tt * 2048 + kh * 1024 + lane) * 16;
#define ISSUE_STAGE(p) do { \
      glds16(wsrc + (p) * 8192, wdst + (p) * 8192); glds16(wsrc + (p) * 8192 + 1024, wdst + (p) * 8192 + 1024); \
      GLOAD16(xr[4 * (p)], xsrc + (p) * 4096, 0); GLOAD16(xr[4 * (p) + 1], xsrc + (p) * 4096, 1024); \
      GLOAD16(xr[4 * (p) + 2], xsrc + (p) * 4096, 2048); GLOAD16(xr[4 * (p) + 3], xsrc + (p) * 4096, 3072); } while (0)
    ISSUE_STAGE(0);
    __builtin_amdgcn_s_barrier();
    ISSUE_STAGE(1);
#pragma unroll
    for (int r = 0; r < 16; ++r) { ak[r] = 0.f; av[r] = 0.f; }
    if (tid < 4) { u32x4 c = {0u, 0u, 0u, 0u}; if (tid == 0) c = (u32x4){0x3F803F80u, 0x3F803F80u, 0x3F803F80u, 0x3F803F80u}; *(u32x4*)(smem + FB_CONST + tid * 16) = c; }
    const bf2d one2 = {(__bf16)1.0f, (__bf16)1.0f};
#pragma unroll
    for (int p = 0; p < 4; ++p) {
      if (p == 0) asm volatile("s_waitcnt vmcnt(6)" : "+v"(xr[0]), "+v"(xr[1]), "+v"(xr[2]), "+v"(xr[3]) :: "memory");
      if (p == 1) asm volatile("s_waitcnt vmcnt(6)" : "+v"(xr[4]), "+v"(xr[5]), "+v"(xr[6]), "+v"(xr[7]) :: "memory");
      if (p == 2) asm volatile("s_waitcnt vmcnt(11)" : "+v"(xr[8]), "+v"(xr[9]), "+v"(xr[10]), "+v"(xr[11]) :: "memory");
      if (p == 3) asm volatile("s_waitcnt vmcnt(8)" : "+v"(xr[12]), "+v"(xr[13]), "+v"(xr[14]), "+v"(xr[15]),
                               "+v"(cq0), "+v"(cq1), "+v"(ck0), "+v"(ck1), "+v"(cvu) :: "memory");
      RAW_BARRIER();
      const u32x4* sr = (const u32x4*)smem + (kh * 16 + 4 * p) * 128 + lane;
      u32x4 nk = sr[0], nv = sr[64];
#pragma unroll
      for (int i = 0; i < 4; ++i) {
        const bf16x8 fk = __builtin_bit_cast(bf16x8, nk);
        const bf16x8 fv = __builtin_bit_cast(bf16x8, nv);
        if (i < 3) { nk = sr[(i + 1) * 128]; nv = sr[(i + 1) * 128 + 64]; }
        __builtin_amdgcn_sched_barrier(0);
        const u32x4 xu = xr[4 * p + i];
        const bf16x8 x = __builtin_bit_cast(bf16x8, xu);
        ak = MFMA_BF(fk, x, ak);
        __builtin_amdgcn_sched_barrier(0);
        if (p < 2) { if (i < 2) glds16(wsrc + (p + 2) * 8192 + i * 1024, wdst + (p + 2) * 8192 + i * 1024);
                     else GLOAD16(xr[4 * (p + 2) + i], xsrc + (p + 2) * 4096, i * 1024); }
        if (p == 2) glds16(qsrc + i * 1024, qdst + i * 1024);
        if (p == 3) GLOAD16(wo[4 + 2 * i], wp + (1 + (i >> 1)) * 4096, (i & 1) * 2048);
        __builtin_amdgcn_sched_barrier(0);
        av = MFMA_BF(x, fv, av);
        __builtin_amdgcn_sched_barrier(0);
        if (p < 2 && i < 2) GLOAD16(xr[4 * (p + 2) + i], xsrc + (p + 2) * 4096, i * 1024);
        if (p == 1 && i == 3) {
          const char* cs = ws + WS_CS + (hp * 32 + 16 * kh + 4 * h) * 4;
          GLOAD16(cq0, cs, 0); GLOAD16(cq1, cs, 32); GLOAD16(ck0, cs, 2048); GLOAD16(ck1, cs, 2080);
          asm volatile("global_load_dword %0, %1, off" : "=v"(cvu) : "v"(ws + WS_CS + (1024 + hp * 32 + li) * 4) : "memory");
        }
        if (p == 2) GLOAD16(wo[i], wp, i * 1024);
        if (p == 3) GLOAD16(wo[5 + 2 * i], wp + (1 + (i >> 1)) * 4096, (i & 1) * 2048 + 1024);
        __builtin_amdgcn_sched_barrier(0);
#pragma unroll
        for (int k = 0; k < 4; ++k) {
          const unsigned xw = xu[k];
          const bf2d xx = __builtin_bit_cast(bf2d, xw);
          if (k & 1) { s1b = __builtin_amdgcn_fdot2_f32_bf16(xx, one2, s1b, false); s2b = __builtin_amdgcn_fdot2_f32_bf16(xx, xx, s2b, false); }
          else       { s1 = __builtin_amdgcn_fdot2_f32_bf16(xx, one2, s1, false);   s2 = __builtin_amdgcn_fdot2_f32_bf16(xx, xx, s2, false); }
        }
      }
    }
  }
  s1 += s1b; s2 += s2b;
  s1 = sum32(s1); s2 = sum32(s2);
#define Q4(v, o) (f32x4){v[o], v[(o) + 1], v[(o) + 2], v[(o) + 3]}
  float kk[8], kv[8];
  {
    f32x4* xs = (f32x4*)(smem + FB_XCH) + (7 - w) * 448 + lane;
    if (kh) {
      xs[128] = Q4(ak, 0); xs[192] = Q4(ak, 4); xs[256] = Q4(av, 0); xs[320] = Q4(av, 4);
#pragma unroll
      for (int j = 0; j < 8; ++j) { kk[j] = ak[8 + j]; kv[j] = av[8 + j]; }
    } else {
      xs[128] = Q4(ak, 8); xs[192] = Q4(ak, 12); xs[256] = Q4(av, 8); xs[320] = Q4(av, 12);
#pragma unroll
      for (int j = 0; j < 8; ++j) { kk[j] = ak[j]; kv[j] = av[j]; }
    }
    f32x4 t2 = {s1, s2, 0.f, 0.f};
    xs[384] = t2;
  }
  RAW_BARRIER();
  float m, r;
  {
    const f32x4* xs = (const f32x4*)(smem + FB_XCH) + w * 448 + lane;
    const f32x4 st = xs[384], b0 = xs[128], b1 = xs[192], c0 = xs[256], c1 = xs[320];
    ln_stats(s1 + st[0], s2 + st[1], m, r);
    float* mr = (float*)(smem + FB_MR) + w * 64;
    if (h == 0) { mr[li] = m; mr[32 + li] = r; }
    const f32x4 fk0 = __builtin_bit_cast(f32x4, ck0), fk1 = __builtin_bit_cast(f32x4, ck1);
    const float cv = u2f(cvu);
    u32x4 vk, vv;
#pragma unroll
    for (int j = 0; j < 2; ++j) {
      vk[j]     = pack_bf2(r * (kk[2 * j] + b0[2 * j] - m * fk0[2 * j]), r * (kk[2 * j + 1] + b0[2 * j + 1] - m * fk0[2 * j + 1]));
      vk[2 + j] = pack_bf2(r * (kk[4 + 2 * j] + b1[2 * j] - m * fk1[2 * j]), r * (kk[5 + 2 * j] + b1[2 * j + 1] - m * fk1[2 * j + 1]));
    }
    __builtin_amdgcn_raw_buffer_store_b128(vk, rs, (int)WS_KF + (((2 * hp + kh) * 64 + tt) * 64 + lane) * 16, 0, 16);
    const f32x4 m0 = *(const f32x4*)(mr + 16 * kh + 4 * h), m1 = *(const f32x4*)(mr + 16 * kh + 8 + 4 * h);
    const f32x4 r0 = *(const f32x4*)(mr + 32 + 16 * kh + 4 * h), r1 = *(const f32x4*)(mr + 32 + 16 * kh + 8 + 4 * h);
#pragma unroll
    for (int j = 0; j < 2; ++j) {
      vv[j]     = pack_bf2(r0[2 * j] * (kv[2 * j] + c0[2 * j] - m0[2 * j] * cv), r0[2 * j + 1] * (kv[2 * j + 1] + c0[2 * j + 1] - m0[2 * j + 1] * cv));
      vv[2 + j] = pack_bf2(r1[2 * j] * (kv[4 + 2 * j] + c1[2 * j] - m1[2 * j] * cv), r1[2 * j + 1] * (kv[5 + 2 * j] + c1[2 * j + 1] - m1[2 * j + 1] * cv));
    }
    const int vh = li >> 4, vl = (li & 15) + 16 * h;
    __builtin_amdgcn_raw_buffer_store_b128(vv, rs, (int)WS_VF + ((((2 * hp + vh) * 64 + tt) * 2 + kh) * 32 + vl) * 16, 0, 16);
    *(u32x4*)(smem + FB_KV + ((8 + ts) * 2 + kh) * 2048 + lane * 16) = vk;
    *(u32x4*)(smem + FB_KV + ((8 + ts) * 2 + vh) * 2048 + 1024 + kh * 512 + vl * 16) = vv;
  }
  asm volatile("s_waitcnt vmcnt(0)" : "+v"(wo[0]), "+v"(wo[1]), "+v"(wo[2]), "+v"(wo[3]), "+v"(wo[4]), "+v"(wo[5]), "+v"(wo[6]), "+v"(wo[7]),
               "+v"(wo[8]), "+v"(wo[9]), "+v"(wo[10]), "+v"(wo[11]) :: "memory");
  if (lane == 0) __hip_atomic_store(flg + (rt * 16 + hp) * 64 + w, 1u, RLX_AGENT);
  RAW_BARRIER();
  STAMP(2, 3);
  bf16x8 q;
  {
    f32x16 aq;
#pragma unroll
    for (int rr = 0; rr < 16; ++rr) aq[rr] = 0.f;
    const u32x4* sq = (const u32x4*)(smem + 65536) + kh * 1024 + lane;
    u32x4 fq[4];
#pragma unroll
    for (int i = 0; i < 4; ++i) fq[i] = sq[i * 64];
#pragma unroll
    for (int i = 0; i < 16; ++i) {
      const u32x4 f = fq[i & 3];
      if (i < 12) fq[i & 3] = sq[(i + 4) * 64];
      aq = MFMA_BF(__builtin_bit_cast(bf16x8, f), __builtin_bit_cast(bf16x8, xr[i]), aq);
      if (i < 4) { __builtin_amdgcn_sched_barrier(0); GLOAD16(wo[12 + i], wp + 3 * 4096, i * 1024); __builtin_amdgcn_sched_barrier(0); }
      if (i == 4) { __builtin_amdgcn_sched_barrier(0); GLOAD16(xres, ws + WS_XF + ((size_t)(tt5 * 32 + 2 * dtile + kh) * 64 + lane) * 16, 0); __builtin_amdgcn_sched_barrier(0); }
    }
    float kq[8];
    f32x4* xs = (f32x4*)(smem + FB_XCH) + (7 - w) * 448 + lane;
    if (kh) {
      xs[0] = Q4(aq, 0); xs[64] = Q4(aq, 4);
#pragma unroll
      for (int j = 0; j < 8; ++j) kq[j] = aq[8 + j];
    } else {
      xs[0] = Q4(aq, 8); xs[64] = Q4(aq, 12);
#pragma unroll
      for (int j = 0; j < 8; ++j) kq[j] = aq[j];
    }
    const bool fetcher = rt - 2 + kh >= 0;
    u32x4 fv[4];
    if (fetcher) {
      wait_all(flg + ((rt - 2 + kh) * 16 + hp) * 64, 8, lane);
      asm volatile("" ::: "memory");
      const int kt = 4 * (rt - 2) + w;
#pragma unroll
      for (int e = 0; e < 2; ++e) {
        fv[2 * e]     = __builtin_amdgcn_raw_buffer_load_b128(rs, (int)WS_KF + (((2 * hp + e) * 64 + kt) * 64 + lane) * 16, 0, 16);
        fv[2 * e + 1] = __builtin_amdgcn_raw_buffer_load_b128(rs, (int)WS_VF + (((2 * hp + e) * 64 + kt) * 64 + lane) * 16, 0, 16);
      }
    }
    RAW_BARRIER();
    const f32x4* xi = (const f32x4*)(smem + FB_XCH) + w * 448 + lane;
    const f32x4 a0 = xi[0], a1 = xi[64];
    const f32x4 fq0 = __builtin_bit_cast(f32x4, cq0), fq1 = __builtin_bit_cast(f32x4, cq1);
    const float rq = r * (0.25f * 1.4426950408889634f);
    u32x4 vq;
#pragma unroll
    for (int j = 0; j < 2; ++j) {
      vq[j]     = pack_bf2(rq * (kq[2 * j] + a0[2 * j] - m * fq0[2 * j]), rq * (kq[2 * j + 1] + a0[2 * j + 1] - m * fq0[2 * j + 1]));
      vq[2 + j] = pack_bf2(rq * (kq[4 + 2 * j] + a1[2 * j] - m * fq1[2 * j]), rq * (kq[5 + 2 * j] + a1[2 * j + 1] - m * fq1[2 * j + 1]));
    }
    q = __builtin_bit_cast(bf16x8, vq);
    if (fetcher) {
#pragma unroll
      for (int e = 0; e < 4; ++e) *(u32x4*)(smem + FB_KV + w * 4096 + e * 1024 + lane * 16) = fv[e];
    }
    RAW_BARRIER();
  }
  STAMP(2, 4);
  {
    const char* kbase = smem + FB_KV + ((ts + 8) * 2 + kh) * 2048 + lane * 16;
    const int vstep = li < 16 ? 4096 : 0; const unsigned vs2 = li < 16 ? 512u : 0u;
    const char* vbase = li < 16 ? smem + FB_KV + ((ts + 8) * 2 + kh) * 2048 + 1024 + ((li & 15) + 16 * h) * 16
                                : smem + FB_CONST + ((li == 16 || li == 20) ? 0 : 16);
    f32x16 o;
    attn_tiles(q, kbase, vbase, vstep, vs2, tt >= 8 ? 9 : tt + 1, li, h, o);
    const float inv = 1.f / o[8];
    u32x4 ov = {pack_bf2(o[0] * inv, o[1] * inv), pack_bf2(o[2] * inv, o[3] * inv),
                pack_bf2(o[4] * inv, o[5] * inv), pack_bf2(o[6] * inv, o[7] * inv)};
    __builtin_amdgcn_raw_buffer_store_b128(ov, rs, (int)WS_OF + ((tt * 32 + 2 * hp + kh) * 64 + lane) * 16, 0, 16);
  }
  asm volatile("s_waitcnt vmcnt(0)" : "+v"(wo[12]), "+v"(wo[13]), "+v"(wo[14]), "+v"(wo[15]), "+v"(xres) :: "memory");
  if (lane == 0) __hip_atomic_store(cntb + tt * 64 + 2 * hp + kh, 1u, RLX_AGENT);
  STAMP(2, 5);
  if (w == 0) wait_all(cntb + tt5 * 64, 32, lane);
  RAW_BARRIER();
  {
    u32x4 v[4];
#pragma unroll
    for (int e = 0; e < 4; ++e) v[e] = __builtin_amdgcn_raw_buffer_load_b128(rs, (int)WS_OF + (tt5 * 2048 + e * 512 + tid) * 16, 0, 16);
#pragma unroll
    for (int e = 0; e < 4; ++e) *(u32x4*)(smem + FB_XCH + (e * 512 + tid) * 16) = v[e];
  }
  RAW_BARRIER();
  STAMP(2, 6);
  f32x16 acc;
#pragma unroll
  for (int r = 0; r < 16; ++r) acc[r] = 0.f;
  {
    const bf16x8* so = (const bf16x8*)(smem + FB_XCH) + kh * 1024 + lane;
#pragma unroll
    for (int i = 0; i < 16; ++i) acc = MFMA_BF(__builtin_bit_cast(bf16x8, wo[i]), so[i * 64], acc);
  }
  float xo[8];
  {
    f32x4* xs = (f32x4*)(smem + FB_XCH + 32768);
    f32x4 t0, t1;
#pragma unroll
    for (int j = 0; j < 4; ++j) { t0[j] = kh ? acc[j] : acc[8 + j]; t1[j] = kh ? acc[4 + j] : acc[12 + j]; }
    xs[(7 - w) * 128 + lane] = t0; xs[(7 - w) * 128 + 64 + lane] = t1;
    RAW_BARRIER();
    const f32x4 a0 = xs[w * 128 + lane], a1 = xs[w * 128 + 64 + lane];
#pragma unroll
    for (int j = 0; j < 4; ++j) {
      const float y0 = (kh ? acc[8 + j] : acc[j]) + a0[j], y1 = (kh ? acc[12 + j] : acc[4 + j]) + a1[j];
      const unsigned xa = xres[j >> 1], xb = xres[2 + (j >> 1)];
      xo[j]     = bfr(((j & 1) ? bfhi(xa) : bflo(xa)) + bfr(y0));
      xo[4 + j] = bfr(((j & 1) ? bfhi(xb) : bflo(xb)) + bfr(y1));
    }
  }
  const int t = tt5 * 32 + li;
  const int dbase = dtile * 32 + 16 * kh + 4 * h;
  if (FINAL) {
    u32x2 v0 = {pack_bf2(xo[0], xo[1]), pack_bf2(xo[2], xo[3])}, v1 = {pack_bf2(xo[4], xo[5]), pack_bf2(xo[6], xo[7])};
    char* ob = smem + FB_XCH;
    *(u32x2*)(ob + li * 272 + (ts * 32 + 16 * kh + 4 * h) * 2) = v0;
    *(u32x2*)(ob + li * 272 + (ts * 32 + 16 * kh + 4 * h) * 2 + 16) = v1;
    RAW_BARRIER();
    const u32x4 ov = *(const u32x4*)(ob + (tid >> 4) * 272 + (tid & 15) * 16);
    const __amdgpu_buffer_rsrc_t ro = __builtin_amdgcn_make_buffer_rsrc(out, 0, L_ * D_ * 2, 0x00020000);
    __builtin_amdgcn_raw_buffer_store_b128(ov, ro, ((tt5 * 32 + (tid >> 4)) * D_ + (hp >> 2) * 128 + (tid & 15) * 8) * 2, 0, 16);
  } else {
    float p1 = 0.f, p2 = 0.f;
#pragma unroll
    for (int j = 0; j < 8; ++j) {
      __builtin_amdgcn_raw_buffer_store_b16((u16)(f2u(xo[j]) >> 16), rs, (int)WS_XT2 + ((dbase + (j & 3) + 8 * (j >> 2)) * L_ + t) * 2, 0, 16);
      p1 += xo[j]; p2 = fmaf(xo[j], xo[j], p2);
    }
    p1 = sum32(p1); p2 = sum32(p2);
    f32x2* sr = (f32x2*)(smem + FB_MR);
    if (h == 0) { f32x2 v = {p1, p2}; sr[w * 32 + li] = v; }
    RAW_BARRIER();
    if (tid < 32) {
      f32x2 v = sr[tid];
#pragma unroll
      for (int k = 1; k < 8; ++k) v += sr[k * 32 + tid];
      __builtin_amdgcn_raw_buffer_store_b64(__builtin_bit_cast(u32x2, v), rs, (int)WS_ST2 + ((hp >> 2) * 2048 + tt5 * 32 + tid) * 8, 0, 16);
    }
  }
  STAMP(2, 7);
}

extern "C" void kernel_launch(void* const* d_in, const int* in_sizes, int n_in,
                              void* d_out, int out_size, void* d_ws, size_t ws_size,
                              hipStream_t stream) {
  const u16* x0 = (const u16*)d_in[0];
  const u16* dskip = (const u16*)d_in[8];
  char* ws = (char*)d_ws;
  (void)hipFuncSetAttribute((const void*)k0_prep, hipFuncAttributeMaxDynamicSharedMemorySize, K0_LDS);
  (void)hipFuncSetAttribute((const void*)k_conv, hipFuncAttributeMaxDynamicSharedMemorySize, CV_LDS);
  (void)hipFuncSetAttribute((const void*)k_blk, hipFuncAttributeMaxDynamicSharedMemorySize, FB_LDS);
  k0_prep<<<512, 512, K0_LDS, stream>>>(x0, (const u16*)d_in[1], (const u16*)d_in[2], (const u16*)d_in[3],
                                            (const u16*)d_in[4], (const u16*)d_in[5], (const u16*)d_in[6],
                                            (const u16*)d_in[7], (const u16*)d_in[10], (const u16*)d_in[11], ws);
  k_conv<<<256, 512, CV_LDS, stream>>>((const u16*)(ws + WS_XT0), (const f32x2*)(ws + WS_ST0), dskip, ws, 1);
  k_blk<<<256, 512, FB_LDS, stream>>>(ws, (u16*)d_out, 0);
  k_conv<<<256, 512, CV_LDS, stream>>>((const u16*)(ws + WS_XT2), (const f32x2*)(ws + WS_ST2), dskip, ws, 0);
  k_blk<<<256, 512, FB_LDS, stream>>>(ws, (u16*)d_out, 1);
}
```
